```python
import torch

def _softmax(x, axis=-1):
    return torch.softmax(x, dim=axis)
def _gelu(x):
    return torch.nn.functional.gelu(x, approximate="tanh")
def _silu(x):
    return torch.nn.functional.silu(x)
def _sigmoid(x):
    return torch.sigmoid(x)
def _layernorm(x, axis=-1, eps=1e-5):
    m = x.mean(dim=axis, keepdim=True)
    v = x.var(dim=axis, keepdim=True, unbiased=False)
    return (x - m) / torch.sqrt(v + eps)
def _scatter_add(tgt, idx, src, axis=0):
    return tgt.clone().index_add_(axis, idx, src)
def _segment_sum(src, seg, n):
    out = torch.zeros((n,) + src.shape[1:], dtype=src.dtype)
    return out.index_add_(0, seg, src)
def _pad(x, pw):
    flat = tuple(v for pair in reversed(pw) for v in pair)
    return torch.nn.functional.pad(x, flat)

kernel_name = 'sequential_preLN__sliding_block_cached__dense_activation_squared_relu__decode'

def setup_inputs():

    _w = (lambda a: (lambda t: t.to(torch.bfloat16) if t.is_floating_point() else t)(torch.as_tensor(a)))
    def _A_setup():
        B, Sq, D = 1, 1, 512
        Hq, Hkv, Sk, Dh = 32, 32, 32768, 128
        assert Hq % Hkv == 0, (Hq, Hkv)
        return {
            "x": _w((lambda *s: torch.randn(*s, dtype=torch.bfloat16) * 0.05)(B, Sq, D)),
            "k_cache": _w((lambda *s: torch.randn(*s, dtype=torch.bfloat16) * 3.2)(B, Hkv, Sk, Dh)),
            "v_cache": _w((lambda *s: torch.randn(*s, dtype=torch.bfloat16) * 0.4)(B, Hkv, Sk, Dh)),
            "wq": _w((lambda *s: torch.randn(*s, dtype=torch.bfloat16) * 3.2)(D, Hq * Dh) * (D ** -0.5)),
            "wo": _w((lambda *s: torch.randn(*s, dtype=torch.bfloat16) * 0.4)(Hq * Dh, D) * ((Hq * Dh) ** -0.5)),
        }
    _A_in = _A_setup()

    def _B_setup():
        B, S, D, F = 1, 1, 512, 1376
        return {
            "x": _w((lambda *s: torch.randn(*s, dtype=torch.bfloat16) * 0.05)(B, S, D)),
            "w1": _w((lambda *s: torch.randn(*s, dtype=torch.bfloat16) * 0.05)(D, F)),
            "b1": _w((lambda *s: torch.randn(*s, dtype=torch.bfloat16) * 0.8)(F)),
            "w2": _w((lambda *s: torch.randn(*s, dtype=torch.bfloat16) * 0.05)(F, D)),
            "b2": _w((lambda *s: torch.randn(*s, dtype=torch.bfloat16) * 3.2)(D)),
        }
    _B_in = _B_setup()

    return {
        "A_x": _A_in["x"],
        "A_k_cache": _A_in["k_cache"],
        "A_v_cache": _A_in["v_cache"],
        "A_wq": _A_in["wq"],
        "A_wo": _A_in["wo"],
        "B_x": _B_in["x"],
        "B_w1": _B_in["w1"],
        "B_b1": _B_in["b1"],
        "B_w2": _B_in["w2"],
        "B_b2": _B_in["b2"],
    }

def reference(A_x, A_k_cache, A_v_cache, A_wq, A_wo, B_x, B_w1, B_b1, B_w2, B_b2):
    def _A_ref(x, k_cache, v_cache, wq, wo):
        B, Sq, D = x.shape
        Hq, Dh = 32, 128
        Hkv, Sk = k_cache.shape[1], k_cache.shape[2]
        W = min(4096, max(256, Sk // 4))
        rep = Hq // Hkv
        q = (lambda x, *d: x.permute(*d))((x @ wq).reshape(B, Sq, Hq, Dh), 0, 2, 1, 3)
        k = (lambda x, n, axis: torch.repeat_interleave(x, n, dim=axis))(k_cache[:, :, Sk - W:, :], rep, axis=1)
        v = (lambda x, n, axis: torch.repeat_interleave(x, n, dim=axis))(v_cache[:, :, Sk - W:, :], rep, axis=1)
        scores = torch.einsum("bhqd,bhkd->bhqk", q, k) * (Dh ** -0.5)
        p = _softmax(scores, axis=-1)
        o = torch.einsum("bhqk,bhkd->bhqd", p, v)
        return (lambda x, *d: x.permute(*d))(o, 0, 2, 1, 3).reshape(B, Sq, Hq * Dh) @ wo

    def _B_ref(x, w1, b1, w2, b2):
        h = torch.einsum("bsd,df->bsf", x, w1) + b1
        zero = torch.zeros_like(h)
        r = torch.where(h > zero, h, zero)
        h = r * r
        out = torch.einsum("bsf,fd->bsd", h, w2) + b2
        return out

    x = A_x
    h = x + _A_ref(_layernorm(x, axis=-1), A_k_cache, A_v_cache, A_wq, A_wo)
    h2 = h + _B_ref(_layernorm(h, axis=-1), B_w1, B_b1, B_w2, B_b2)
    return h2
```

```cpp
#include <hip/hip_runtime.h>
#include <hip/hip_bf16.h>
#include <stdint.h>

typedef unsigned u32x4 __attribute__((ext_vector_type(4)));
typedef float f32x4 __attribute__((ext_vector_type(4)));
typedef __bf16 bf16x2v __attribute__((ext_vector_type(2)));
typedef unsigned short u16;

constexpr int DM = 512, HQ = 32, DH = 128, SK = 32768, WIN = 4096, FF = 1376;
constexpr int NSPLIT = 8, KPS = WIN / NSPLIT;
constexpr float SCALE = 0.08838834764831845f;
constexpr float EPS = 1e-5f;
constexpr int PREC = 132;

__device__ __forceinline__ float bf_lo(unsigned u) { return __uint_as_float(u << 16); }
__device__ __forceinline__ float bf_hi(unsigned u) { return __uint_as_float(u & 0xffff0000u); }
__device__ __forceinline__ float bf1(u16 u) { return __uint_as_float(((unsigned)u) << 16); }
__device__ __forceinline__ float bfr(float x) { return (float)(__bf16)x; }
__device__ __forceinline__ u16 bfbits(float x) { return __builtin_bit_cast(u16, (__bf16)x); }
__device__ __forceinline__ float dot2(unsigned a, unsigned b, float acc) {
  return __builtin_amdgcn_fdot2_f32_bf16(__builtin_bit_cast(bf16x2v, a), __builtin_bit_cast(bf16x2v, b), acc, false);
}
template <int CTRL> __device__ __forceinline__ float dpp(float x) {
  return __builtin_bit_cast(float, __builtin_amdgcn_mov_dpp(__builtin_bit_cast(int, x), CTRL, 0xf, 0xf, true));
}
__device__ __forceinline__ float row16_sum(float x) {
  x += dpp<0x128>(x); x += dpp<0x124>(x); x += dpp<0x122>(x); x += dpp<0x121>(x); return x;
}
__device__ __forceinline__ float row16_max(float x) {
  x = fmaxf(x, dpp<0x128>(x)); x = fmaxf(x, dpp<0x124>(x)); x = fmaxf(x, dpp<0x122>(x)); x = fmaxf(x, dpp<0x121>(x)); return x;
}
__device__ __forceinline__ float xr_sum(float x) { x += __shfl_xor(x, 16); x += __shfl_xor(x, 32); return x; }
__device__ __forceinline__ float xr_max(float x) { x = fmaxf(x, __shfl_xor(x, 16)); x = fmaxf(x, __shfl_xor(x, 32)); return x; }
__device__ __forceinline__ float wave_sum(float x) { return xr_sum(row16_sum(x)); }

__device__ __forceinline__ void ln_stats8(const float (&v)[8], float& m_bf, float& den) {
  float s = 0.f;
#pragma unroll
  for (int e = 0; e < 8; ++e) s += v[e];
  s = wave_sum(s);
  const float mean = s * (1.0f / 512.0f);
  float q = 0.f;
#pragma unroll
  for (int e = 0; e < 8; ++e) { const float d = v[e] - mean; q += d * d; }
  q = wave_sum(q);
  const float var = q * (1.0f / 512.0f);
  m_bf = bfr(mean);
  den = bfr(sqrtf(bfr(bfr(var) + EPS)));
}

__global__ void __launch_bounds__(512, 2) k_attn(const u16* __restrict__ x, const u16* __restrict__ kc, const u16* __restrict__ vc,
                                                 const u16* __restrict__ wq, float* __restrict__ part) {
  __shared__ __attribute__((aligned(16))) float s_ln[8][64];
  __shared__ __attribute__((aligned(16))) float s_q[8][128];
  __shared__ __attribute__((aligned(16))) float s_o[8][128];
  __shared__ float s_m[8], s_l[8];
  const int b = blockIdx.x, h = b & 31, s = b >> 5;
  const int tid = threadIdx.x, lane = tid & 63, w = tid >> 6, r = lane >> 4, c = lane & 15;

  const u32x4 xv = reinterpret_cast<const u32x4*>(x)[lane];
  const u32x4* wqp = reinterpret_cast<const u32x4*>(wq + (size_t)(64 * w + r) * 4096 + h * 128 + 8 * c);
  u32x4 wv[16];
#pragma unroll
  for (int t = 0; t < 16; ++t) wv[t] = wqp[(size_t)t * (4 * 4096 / 8)];
  const u32x4* kp = reinterpret_cast<const u32x4*>(kc + ((size_t)h * SK + (SK - WIN) + s * KPS + 64 * w + r) * DH + 8 * c);
  u32x4 kv[16];
#pragma unroll
  for (int t = 0; t < 16; ++t) kv[t] = __builtin_nontemporal_load(kp + (size_t)t * (4 * DH / 8));

  float xf[8];
#pragma unroll
  for (int e = 0; e < 4; ++e) { xf[2 * e] = bf_lo(xv[e]); xf[2 * e + 1] = bf_hi(xv[e]); }
  float m_bf, den;
  ln_stats8(xf, m_bf, den);
  if ((lane >> 3) == w) {
#pragma unroll
    for (int e = 0; e < 8; ++e) s_ln[w][(lane & 7) * 8 + e] = bfr(bfr(xf[e] - m_bf) / den);
  }
  __builtin_amdgcn_wave_barrier();
  float acc[8];
#pragma unroll
  for (int e = 0; e < 8; ++e) acc[e] = 0.f;
#pragma unroll
  for (int t = 0; t < 16; ++t) {
    const float lv = s_ln[w][4 * t + r];
#pragma unroll
    for (int e = 0; e < 4; ++e) { acc[2 * e] = fmaf(lv, bf_lo(wv[t][e]), acc[2 * e]); acc[2 * e + 1] = fmaf(lv, bf_hi(wv[t][e]), acc[2 * e + 1]); }
  }
#pragma unroll
  for (int e = 0; e < 8; ++e) acc[e] = xr_sum(acc[e]);
  if (r == 0) {
    *reinterpret_cast<f32x4*>(&s_q[w][8 * c]) = f32x4{acc[0], acc[1], acc[2], acc[3]};
    *reinterpret_cast<f32x4*>(&s_q[w][8 * c + 4]) = f32x4{acc[4], acc[5], acc[6], acc[7]};
  }
  const u32x4* vp = reinterpret_cast<const u32x4*>(vc + ((size_t)h * SK + (SK - WIN) + s * KPS + 64 * w + r) * DH + 8 * c);
  u32x4 vv[16];
#pragma unroll
  for (int t = 0; t < 16; ++t) vv[t] = __builtin_nontemporal_load(vp + (size_t)t * (4 * DH / 8));
  __syncthreads();
  float qf[8];
#pragma unroll
  for (int e = 0; e < 8; ++e) qf[e] = 0.f;
#pragma unroll
  for (int ww = 0; ww < 8; ++ww) {
    const f32x4 a = *reinterpret_cast<const f32x4*>(&s_q[ww][8 * c]);
    const f32x4 bq = *reinterpret_cast<const f32x4*>(&s_q[ww][8 * c + 4]);
#pragma unroll
    for (int e = 0; e < 4; ++e) { qf[e] += a[e]; qf[4 + e] += bq[e]; }
  }
  unsigned qpk[4];
#pragma unroll
  for (int e = 0; e < 4; ++e) qpk[e] = (unsigned)bfbits(qf[2 * e]) | ((unsigned)bfbits(qf[2 * e + 1]) << 16);
  float sc[16];
#pragma unroll
  for (int t = 0; t < 16; ++t) {
    float d = 0.f;
#pragma unroll
    for (int e = 0; e < 4; ++e) d = dot2(kv[t][e], qpk[e], d);
    d = row16_sum(d);
    sc[t] = bfr(bfr(d) * SCALE);
  }
  float mx = sc[0];
#pragma unroll
  for (int t = 1; t < 16; ++t) mx = fmaxf(mx, sc[t]);
  mx = xr_max(mx);
  float ls = 0.f;
  float oa[8];
#pragma unroll
  for (int e = 0; e < 8; ++e) oa[e] = 0.f;
#pragma unroll
  for (int t = 0; t < 16; ++t) {
    const float p = __builtin_amdgcn_exp2f((sc[t] - mx) * 1.4426950408889634f);
    ls += p;
#pragma unroll
    for (int e = 0; e < 4; ++e) { oa[2 * e] = fmaf(p, bf_lo(vv[t][e]), oa[2 * e]); oa[2 * e + 1] = fmaf(p, bf_hi(vv[t][e]), oa[2 * e + 1]); }
  }
  ls = xr_sum(ls);
#pragma unroll
  for (int e = 0; e < 8; ++e) oa[e] = xr_sum(oa[e]);
  if (r == 0) {
    *reinterpret_cast<f32x4*>(&s_o[w][8 * c]) = f32x4{oa[0], oa[1], oa[2], oa[3]};
    *reinterpret_cast<f32x4*>(&s_o[w][8 * c + 4]) = f32x4{oa[4], oa[5], oa[6], oa[7]};
    if (c == 0) { s_m[w] = mx; s_l[w] = ls; }
  }
  __syncthreads();
  if (tid < 128) {
    float M = s_m[0];
#pragma unroll
    for (int ww = 1; ww < 8; ++ww) M = fmaxf(M, s_m[ww]);
    float L = 0.f, o = 0.f;
#pragma unroll
    for (int ww = 0; ww < 8; ++ww) {
      const float e = __builtin_amdgcn_exp2f((s_m[ww] - M) * 1.4426950408889634f);
      L = fmaf(e, s_l[ww], L); o = fmaf(e, s_o[ww][tid], o);
    }
    float* rec = part + (size_t)(h * NSPLIT + s) * PREC;
    rec[4 + tid] = o;
    if (tid == 0) { rec[0] = M; rec[1] = L; }
  }
}

__global__ void __launch_bounds__(512) k_wo(const float* __restrict__ part, const u16* __restrict__ wo, float* __restrict__ y) {
  __shared__ float s_o[128];
  __shared__ float s_red[8][64];
  const int b = blockIdx.x, h = b & 31, s = b >> 5;
  const int tid = threadIdx.x, lane = tid & 63, w = tid >> 6;
  u32x4 wv[2];
#pragma unroll
  for (int i = 0; i < 2; ++i)
    wv[i] = *reinterpret_cast<const u32x4*>(wo + (size_t)(h * 128 + 16 * w + 8 * i + (lane >> 3)) * DM + 64 * s + 8 * (lane & 7));
  if (tid < 128) {
    const float* p = part + (size_t)h * NSPLIT * PREC;
    float M = p[0];
#pragma unroll
    for (int ss = 1; ss < NSPLIT; ++ss) M = fmaxf(M, p[ss * PREC]);
    float L = 0.f, o = 0.f;
#pragma unroll
    for (int ss = 0; ss < NSPLIT; ++ss) {
      const float e = __builtin_amdgcn_exp2f((p[ss * PREC] - M) * 1.4426950408889634f);
      L = fmaf(e, p[ss * PREC + 1], L); o = fmaf(e, p[ss * PREC + 4 + tid], o);
    }
    s_o[tid] = bfr(o / L);
  }
  __syncthreads();
  float acc[8];
#pragma unroll
  for (int e = 0; e < 8; ++e) acc[e] = 0.f;
#pragma unroll
  for (int i = 0; i < 2; ++i) {
    const float ov = s_o[16 * w + 8 * i + (lane >> 3)];
#pragma unroll
    for (int e = 0; e < 4; ++e) { acc[2 * e] = fmaf(ov, bf_lo(wv[i][e]), acc[2 * e]); acc[2 * e + 1] = fmaf(ov, bf_hi(wv[i][e]), acc[2 * e + 1]); }
  }
#pragma unroll
  for (int e = 0; e < 8; ++e) { acc[e] += __shfl_xor(acc[e], 8); acc[e] += __shfl_xor(acc[e], 16); acc[e] += __shfl_xor(acc[e], 32); }
  if (lane < 8) {
#pragma unroll
    for (int e = 0; e < 8; ++e) s_red[w][8 * lane + e] = acc[e];
  }
  __syncthreads();
  if (tid < 64) {
    float t = 0.f;
#pragma unroll
    for (int ww = 0; ww < 8; ++ww) t += s_red[ww][tid];
    y[h * DM + 64 * s + tid] = t;
  }
}

__global__ void __launch_bounds__(512) k_mlp(const u16* __restrict__ x, const float* __restrict__ y, const u16* __restrict__ w1,
                                             const u16* __restrict__ b1, const u16* __restrict__ w2, float* __restrict__ hvec, float* __restrict__ outp) {
  __shared__ float s_red[8][8];
  __shared__ float s_st[2][8];
  __shared__ float s_u[8];
  __shared__ __attribute__((aligned(16))) float s_o[8][512];
  const int j = blockIdx.x, tid = threadIdx.x, lane = tid & 63, w = tid >> 6;
  const u32x4 w1v = *reinterpret_cast<const u32x4*>(w1 + (size_t)tid * FF + 8 * j);
  const u32x4 w2v = *reinterpret_cast<const u32x4*>(w2 + (size_t)(8 * j) * DM + 8 * tid);
  float a = 0.f;
#pragma unroll
  for (int hh = 0; hh < HQ; ++hh) a += y[hh * DM + tid];
  const float hv = bfr(bf1(x[tid]) + bfr(a));
  float sm = wave_sum(hv);
  if (lane == 0) s_st[0][w] = sm;
  __syncthreads();
  float tot = 0.f;
#pragma unroll
  for (int ww = 0; ww < 8; ++ww) tot += s_st[0][ww];
  const float mean = tot * (1.0f / 512.0f);
  const float dd = hv - mean;
  float q = wave_sum(dd * dd);
  if (lane == 0) s_st[1][w] = q;
  __syncthreads();
  float qt = 0.f;
#pragma unroll
  for (int ww = 0; ww < 8; ++ww) qt += s_st[1][ww];
  const float var = qt * (1.0f / 512.0f);
  const float m_bf = bfr(mean), den = bfr(sqrtf(bfr(bfr(var) + EPS)));
  const float ln = bfr(bfr(hv - m_bf) / den);
  float acc[8];
#pragma unroll
  for (int e = 0; e < 4; ++e) { acc[2 * e] = ln * bf_lo(w1v[e]); acc[2 * e + 1] = ln * bf_hi(w1v[e]); }
#pragma unroll
  for (int e = 0; e < 8; ++e) acc[e] = wave_sum(acc[e]);
  if (lane == 0) {
#pragma unroll
    for (int e = 0; e < 8; ++e) s_red[w][e] = acc[e];
  }
  __syncthreads();
  if (tid < 8) {
    float u = 0.f;
#pragma unroll
    for (int ww = 0; ww < 8; ++ww) u += s_red[ww][tid];
    u = bfr(bfr(u) + bf1(b1[8 * j + tid]));
    const float rr = u > 0.f ? u : 0.f;
    s_u[tid] = bfr(rr * rr);
  }
  __syncthreads();
  {
    const float uu = s_u[w];
    f32x4 lo, hi;
#pragma unroll
    for (int e = 0; e < 2; ++e) { lo[2 * e] = uu * bf_lo(w2v[e]); lo[2 * e + 1] = uu * bf_hi(w2v[e]); hi[2 * e] = uu * bf_lo(w2v[2 + e]); hi[2 * e + 1] = uu * bf_hi(w2v[2 + e]); }
    *reinterpret_cast<f32x4*>(&s_o[w][8 * lane]) = lo;
    *reinterpret_cast<f32x4*>(&s_o[w][8 * lane + 4]) = hi;
  }
  __syncthreads();
  float t = 0.f;
#pragma unroll
  for (int ww = 0; ww < 8; ++ww) t += s_o[ww][tid];
  outp[(size_t)j * DM + tid] = t;
  if (j == 0) hvec[tid] = hv;
}

__global__ void __launch_bounds__(256) k_fin(const float* __restrict__ hvec, const float* __restrict__ outp, const u16* __restrict__ b2, u16* __restrict__ out) {
  __shared__ float s_r[8][32];
  const int tid = threadIdx.x, cc = tid & 31, pt = tid >> 5, col = 32 * blockIdx.x + cc;
  float t = 0.f;
  for (int j = pt; j < FF / 8; j += 8) t += outp[(size_t)j * DM + col];
  s_r[pt][cc] = t;
  __syncthreads();
  if (tid < 32) {
    float a = 0.f;
#pragma unroll
    for (int p = 0; p < 8; ++p) a += s_r[p][tid];
    const float o = bfr(bfr(a) + bf1(b2[col]));
    out[col] = bfbits(hvec[col] + o);
  }
}

extern "C" void kernel_launch(void* const* d_in, const int* in_sizes, int n_in,
                              void* d_out, int out_size, void* d_ws, size_t ws_size,
                              hipStream_t stream) {
  const u16* x  = (const u16*)d_in[0];
  const u16* kc = (const u16*)d_in[1];
  const u16* vc = (const u16*)d_in[2];
  const u16* wq = (const u16*)d_in[3];
  const u16* wo = (const u16*)d_in[4];
  const u16* w1 = (const u16*)d_in[6];
  const u16* b1 = (const u16*)d_in[7];
  const u16* w2 = (const u16*)d_in[8];
  const u16* b2 = (const u16*)d_in[9];
  char* ws = (char*)d_ws;
  float* part = (float*)(ws);
  float* y    = (float*)(ws + (256 << 10));
  float* hvec = (float*)(ws + (512 << 10));
  float* outp = (float*)(ws + (1 << 20));
  k_attn<<<256, 512, 0, stream>>>(x, kc, vc, wq, part);
  k_wo<<<256, 512, 0, stream>>>(part, wo, y);
  k_mlp<<<FF / 8, 512, 0, stream>>>(x, y, w1, b1, w2, hvec, outp);
  k_fin<<<DM / 32, 256, 0, stream>>>(hvec, outp, b2, (u16*)d_out);
}
```

```cpp
#include <hip/hip_runtime.h>
#include <hip/hip_bf16.h>
#include <stdint.h>

typedef unsigned u32x4 __attribute__((ext_vector_type(4)));
typedef float f32x4 __attribute__((ext_vector_type(4)));
typedef __bf16 bf16x2v __attribute__((ext_vector_type(2)));
typedef unsigned short u16;

constexpr int DM = 512, HQ = 32, DH = 128, SK = 32768, WIN = 4096, FF = 1376;
constexpr int NSPLIT = 8, KPS = WIN / NSPLIT;
constexpr float SCALE = 0.08838834764831845f;
constexpr float EPS = 1e-5f;
constexpr int PREC = 132;

__device__ __forceinline__ float bf_lo(unsigned u) { return __uint_as_float(u << 16); }
__device__ __forceinline__ float bf_hi(unsigned u) { return __uint_as_float(u & 0xffff0000u); }
__device__ __forceinline__ float bf1(u16 u) { return __uint_as_float(((unsigned)u) << 16); }
__device__ __forceinline__ float bfr(float x) { return (float)(__bf16)x; }
__device__ __forceinline__ u16 bfbits(float x) { return __builtin_bit_cast(u16, (__bf16)x); }
__device__ __forceinline__ float dot2(unsigned a, unsigned b, float acc) {
  return __builtin_amdgcn_fdot2_f32_bf16(__builtin_bit_cast(bf16x2v, a), __builtin_bit_cast(bf16x2v, b), acc, false);
}
template <int CTRL> __device__ __forceinline__ float dpp(float x) {
  return __builtin_bit_cast(float, __builtin_amdgcn_mov_dpp(__builtin_bit_cast(int, x), CTRL, 0xf, 0xf, true));
}
__device__ __forceinline__ float row16_sum(float x) {
  x += dpp<0x128>(x); x += dpp<0x124>(x); x += dpp<0x122>(x); x += dpp<0x121>(x); return x;
}
__device__ __forceinline__ float row16_max(float x) {
  x = fmaxf(x, dpp<0x128>(x)); x = fmaxf(x, dpp<0x124>(x)); x = fmaxf(x, dpp<0x122>(x)); x = fmaxf(x, dpp<0x121>(x)); return x;
}
__device__ __forceinline__ float xr_sum(float x) { x += __shfl_xor(x, 16); x += __shfl_xor(x, 32); return x; }
__device__ __forceinline__ float xr_max(float x) { x = fmaxf(x, __shfl_xor(x, 16)); x = fmaxf(x, __shfl_xor(x, 32)); return x; }
__device__ __forceinline__ float wave_sum(float x) { return xr_sum(row16_sum(x)); }

__device__ __forceinline__ void ln_stats8(const float (&v)[8], float& m_bf, float& den) {
  float s = 0.f;
#pragma unroll
  for (int e = 0; e < 8; ++e) s += v[e];
  s = wave_sum(s);
  const float mean = s * (1.0f / 512.0f);
  float q = 0.f;
#pragma unroll
  for (int e = 0; e < 8; ++e) { const float d = v[e] - mean; q += d * d; }
  q = wave_sum(q);
  const float var = q * (1.0f / 512.0f);
  m_bf = bfr(mean);
  den = bfr(sqrtf(bfr(bfr(var) + EPS)));
}

#define KPRE 0
#define ST(i) do {} while (0)
#define STAMP_PARAM
__global__ void __launch_bounds__(512, 2) k_attn(const u16* __restrict__ x, const u16* __restrict__ kc, const u16* __restrict__ vc,
                                                 const u16* __restrict__ wq, float* __restrict__ part, unsigned* __restrict__ cnt STAMP_PARAM) {
  ST(0);
  __shared__ __attribute__((aligned(16))) float s_ln[8][64];
  __shared__ __attribute__((aligned(16))) float s_q[8][128];
  __shared__ __attribute__((aligned(16))) float s_o[8][128];
  __shared__ float s_m[8], s_l[8];
  const int b = blockIdx.x, h = b & 31, s = b >> 5;
  const int tid = threadIdx.x, lane = tid & 63, w = tid >> 6, r = lane >> 4, c = lane & 15;

  if (b < 4) cnt[b * 512 + tid] = 0u;
  const u32x4 xv = reinterpret_cast<const u32x4*>(x)[lane];
  const u32x4* kp = reinterpret_cast<const u32x4*>(kc + ((size_t)h * SK + (SK - WIN) + s * KPS + 64 * w + r) * DH + 8 * c);
  const u32x4* vp = reinterpret_cast<const u32x4*>(vc + ((size_t)h * SK + (SK - WIN) + s * KPS + 64 * w + r) * DH + 8 * c);
  const u32x4* wqp = reinterpret_cast<const u32x4*>(wq + (size_t)(64 * w + r) * 4096 + h * 128 + 8 * c);
  u32x4 wv[16], kv[16], vv[16];
#define ISSUE_KV(j) do { if ((j) < 16) kv[(j) & 15] = __builtin_nontemporal_load(kp + (size_t)((j) & 15) * (4 * DH / 8)); \
                         else if ((j) < 32) vv[(j) & 15] = __builtin_nontemporal_load(vp + (size_t)((j) & 15) * (4 * DH / 8)); } while (0)
#pragma unroll
  for (int t = 0; t < KPRE; ++t) ISSUE_KV(t);
#pragma unroll
  for (int t = 0; t < 16; ++t) wv[t] = __builtin_nontemporal_load(wqp + (size_t)t * (4 * 4096 / 8));
  __builtin_amdgcn_sched_barrier(0);

  ST(1);
  float xf[8];
#pragma unroll
  for (int e = 0; e < 4; ++e) { xf[2 * e] = bf_lo(xv[e]); xf[2 * e + 1] = bf_hi(xv[e]); }
  float m_bf, den;
  ln_stats8(xf, m_bf, den);
  if ((lane >> 3) == w) {
#pragma unroll
    for (int e = 0; e < 8; ++e) s_ln[w][(lane & 7) * 8 + e] = bfr(bfr(xf[e] - m_bf) / den);
  }
  __builtin_amdgcn_wave_barrier();
  ST(2);
  float acc[8];
#pragma unroll
  for (int e = 0; e < 8; ++e) acc[e] = 0.f;
#pragma unroll
  for (int t = 0; t < 16; ++t) {
    const float lv = s_ln[w][4 * t + r];
#pragma unroll
    for (int e = 0; e < 4; ++e) { acc[2 * e] = fmaf(lv, bf_lo(wv[t][e]), acc[2 * e]); acc[2 * e + 1] = fmaf(lv, bf_hi(wv[t][e]), acc[2 * e + 1]); }
    ISSUE_KV(KPRE + t);
    __builtin_amdgcn_sched_barrier(0);
  }
#pragma unroll
  for (int e = 0; e < 8; ++e) acc[e] = xr_sum(acc[e]);
  if (r == 0) {
    *reinterpret_cast<f32x4*>(&s_q[w][8 * c]) = f32x4{acc[0], acc[1], acc[2], acc[3]};
    *reinterpret_cast<f32x4*>(&s_q[w][8 * c + 4]) = f32x4{acc[4], acc[5], acc[6], acc[7]};
  }
  ST(3);
  __syncthreads();
  float qf[8];
#pragma unroll
  for (int e = 0; e < 8; ++e) qf[e] = 0.f;
#pragma unroll
  for (int ww = 0; ww < 8; ++ww) {
    const f32x4 a = *reinterpret_cast<const f32x4*>(&s_q[ww][8 * c]);
    const f32x4 bq = *reinterpret_cast<const f32x4*>(&s_q[ww][8 * c + 4]);
#pragma unroll
    for (int e = 0; e < 4; ++e) { qf[e] += a[e]; qf[4 + e] += bq[e]; }
  }
  unsigned qpk[4];
#pragma unroll
  for (int e = 0; e < 4; ++e) qpk[e] = (unsigned)bfbits(qf[2 * e]) | ((unsigned)bfbits(qf[2 * e + 1]) << 16);
  ST(4);
  float sc[16];
#pragma unroll
  for (int t = 0; t < 16; ++t) {
    float d = 0.f;
#pragma unroll
    for (int e = 0; e < 4; ++e) d = dot2(kv[t][e], qpk[e], d);
    d = row16_sum(d);
    sc[t] = bfr(bfr(d) * SCALE);
    ISSUE_KV(KPRE + 16 + t);
    __builtin_amdgcn_sched_barrier(0);
  }
#undef ISSUE_KV
  float mx = sc[0];
#pragma unroll
  for (int t = 1; t < 16; ++t) mx = fmaxf(mx, sc[t]);
  mx = xr_max(mx);
  ST(5);
  float ls = 0.f;
  float oa[8];
#pragma unroll
  for (int e = 0; e < 8; ++e) oa[e] = 0.f;
#pragma unroll
  for (int t = 0; t < 16; ++t) {
    const float p = __builtin_amdgcn_exp2f((sc[t] - mx) * 1.4426950408889634f);
    ls += p;
#pragma unroll
    for (int e = 0; e < 4; ++e) { oa[2 * e] = fmaf(p, bf_lo(vv[t][e]), oa[2 * e]); oa[2 * e + 1] = fmaf(p, bf_hi(vv[t][e]), oa[2 * e + 1]); }
  }
  ST(6);
  ls = xr_sum(ls);
#pragma unroll
  for (int e = 0; e < 8; ++e) oa[e] = xr_sum(oa[e]);
  if (r == 0) {
    *reinterpret_cast<f32x4*>(&s_o[w][8 * c]) = f32x4{oa[0], oa[1], oa[2], oa[3]};
    *reinterpret_cast<f32x4*>(&s_o[w][8 * c + 4]) = f32x4{oa[4], oa[5], oa[6], oa[7]};
    if (c == 0) { s_m[w] = mx; s_l[w] = ls; }
  }
  __syncthreads();
  if (tid < 128) {
    float M = s_m[0];
#pragma unroll
    for (int ww = 1; ww < 8; ++ww) M = fmaxf(M, s_m[ww]);
    float L = 0.f, o = 0.f;
#pragma unroll
    for (int ww = 0; ww < 8; ++ww) {
      const float e = __builtin_amdgcn_exp2f((s_m[ww] - M) * 1.4426950408889634f);
      L = fmaf(e, s_l[ww], L); o = fmaf(e, s_o[ww][tid], o);
    }
    float* rec = part + (size_t)(h * NSPLIT + s) * PREC;
    rec[4 + tid] = o;
    if (tid == 0) { rec[0] = M; rec[1] = L; }
  }
}

#define RLX_AGENT __ATOMIC_RELAXED, __HIP_MEMORY_SCOPE_AGENT
constexpr int CNT_STRIDE = 32;
constexpr int CNT_A = 0, CNT_B = 32 * CNT_STRIDE, CNT_TMO = 48 * CNT_STRIDE, CNT_WORDS = 64 * CNT_STRIDE;
constexpr int NMLP = FF / 8;
constexpr unsigned SPIN_CAP = 1u << 20;
__device__ __forceinline__ float ld_sc1(const float* p) { return __hip_atomic_load(p, RLX_AGENT); }
__device__ __forceinline__ void st_sc1(float* p, float v) { __hip_atomic_store(p, v, RLX_AGENT); }

__global__ void __launch_bounds__(512) k_tail(const float* __restrict__ part, const u16* __restrict__ x, const u16* __restrict__ wo,
                                              const u16* __restrict__ w1, const u16* __restrict__ b1, const u16* __restrict__ w2, const u16* __restrict__ b2,
                                              float* y, float* outp, unsigned* cnt, u16* __restrict__ out) {
  __shared__ __attribute__((aligned(16))) float s_big[8][512];
  __shared__ float s_o[128];
  __shared__ float s_red[8][64];
  __shared__ float s_st[2][8];
  __shared__ float s_u[8];
  const int b = blockIdx.x, h = b & 31, s = b >> 5;
  const int tid = threadIdx.x, lane = tid & 63, w = tid >> 6;
  const bool mlp = b < NMLP;
  u32x4 wv[2];
#pragma unroll
  for (int i = 0; i < 2; ++i)
    wv[i] = *reinterpret_cast<const u32x4*>(wo + (size_t)(h * 128 + 16 * w + 8 * i + (lane >> 3)) * DM + 64 * s + 8 * (lane & 7));
  const int j = mlp ? b : 0;
  const u32x4 w1v = *reinterpret_cast<const u32x4*>(w1 + (size_t)tid * FF + 8 * j);
  const u32x4 w2v = *reinterpret_cast<const u32x4*>(w2 + (size_t)(8 * j) * DM + 8 * tid);
  const float xv = bf1(x[tid]);
  const float b1v = bf1(b1[8 * j + (tid & 7)]);
  if (tid < 128) {
    const float* p = part + (size_t)h * NSPLIT * PREC;
    float M = p[0];
#pragma unroll
    for (int ss = 1; ss < NSPLIT; ++ss) M = fmaxf(M, p[ss * PREC]);
    float L = 0.f, o = 0.f;
#pragma unroll
    for (int ss = 0; ss < NSPLIT; ++ss) {
      const float e = __builtin_amdgcn_exp2f((p[ss * PREC] - M) * 1.4426950408889634f);
      L = fmaf(e, p[ss * PREC + 1], L); o = fmaf(e, p[ss * PREC + 4 + tid], o);
    }
    s_o[tid] = bfr(o / L);
  }
  __syncthreads();
  {
    float acc[8];
#pragma unroll
    for (int e = 0; e < 8; ++e) acc[e] = 0.f;
#pragma unroll
    for (int i = 0; i < 2; ++i) {
      const float ov = s_o[16 * w + 8 * i + (lane >> 3)];
#pragma unroll
      for (int e = 0; e < 4; ++e) { acc[2 * e] = fmaf(ov, bf_lo(wv[i][e]), acc[2 * e]); acc[2 * e + 1] = fmaf(ov, bf_hi(wv[i][e]), acc[2 * e + 1]); }
    }
#pragma unroll
    for (int e = 0; e < 8; ++e) { acc[e] += __shfl_xor(acc[e], 8); acc[e] += __shfl_xor(acc[e], 16); acc[e] += __shfl_xor(acc[e], 32); }
    if (lane < 8) {
#pragma unroll
      for (int e = 0; e < 8; ++e) s_red[w][8 * lane + e] = acc[e];
    }
  }
  __syncthreads();
  if (tid < 64) {
    float t = 0.f;
#pragma unroll
    for (int ww = 0; ww < 8; ++ww) t += s_red[ww][tid];
    st_sc1(&y[h * DM + 64 * s + tid], t);
    asm volatile("s_waitcnt vmcnt(0)" ::: "memory");
    if (tid == 0) __hip_atomic_fetch_add(&cnt[CNT_A + CNT_STRIDE * h], 1u, RLX_AGENT);
  }
  if (!mlp) return;
  if (w == 0) {
    unsigned spins = 0;
    for (;;) {
      const unsigned v = (lane < 32) ? __hip_atomic_load(&cnt[CNT_A + CNT_STRIDE * lane], RLX_AGENT) : 8u;
      if (__all(v >= 8u)) break;
      __builtin_amdgcn_s_sleep(1);
      if (++spins > SPIN_CAP) { if (lane == 0) __hip_atomic_store(&cnt[CNT_TMO], 1u, RLX_AGENT); break; }
    }
  }
  __syncthreads();
  float a = 0.f;
  {
    float yv[HQ];
#pragma unroll
    for (int hh = 0; hh < HQ; ++hh) yv[hh] = ld_sc1(&y[hh * DM + tid]);
#pragma unroll
    for (int hh = 0; hh < HQ; ++hh) a += yv[hh];
  }
  const float hv = bfr(xv + bfr(a));
  float sm = wave_sum(hv);
  if (lane == 0) s_st[0][w] = sm;
  __syncthreads();
  float tot = 0.f;
#pragma unroll
  for (int ww = 0; ww < 8; ++ww) tot += s_st[0][ww];
  const float mean = tot * (1.0f / 512.0f);
  const float dd = hv - mean;
  float q = wave_sum(dd * dd);
  if (lane == 0) s_st[1][w] = q;
  __syncthreads();
  float qt = 0.f;
#pragma unroll
  for (int ww = 0; ww < 8; ++ww) qt += s_st[1][ww];
  const float var = qt * (1.0f / 512.0f);
  const float m_bf = bfr(mean), den = bfr(sqrtf(bfr(bfr(var) + EPS)));
  const float ln = bfr(bfr(hv - m_bf) / den);
  {
    float acc[8];
#pragma unroll
    for (int e = 0; e < 4; ++e) { acc[2 * e] = ln * bf_lo(w1v[e]); acc[2 * e + 1] = ln * bf_hi(w1v[e]); }
#pragma unroll
    for (int e = 0; e < 8; ++e) acc[e] = wave_sum(acc[e]);
    if (lane == 0) {
#pragma unroll
      for (int e = 0; e < 8; ++e) s_red[w][e] = acc[e];
    }
  }
  __syncthreads();
  if (tid < 8) {
    float u = 0.f;
#pragma unroll
    for (int ww = 0; ww < 8; ++ww) u += s_red[ww][tid];
    u = bfr(bfr(u) + b1v);
    const float rr = u > 0.f ? u : 0.f;
    s_u[tid] = bfr(rr * rr);
  }
  __syncthreads();
  {
    const float uu = s_u[w];
    f32x4 lo, hi;
#pragma unroll
    for (int e = 0; e < 2; ++e) { lo[2 * e] = uu * bf_lo(w2v[e]); lo[2 * e + 1] = uu * bf_hi(w2v[e]); hi[2 * e] = uu * bf_lo(w2v[2 + e]); hi[2 * e + 1] = uu * bf_hi(w2v[2 + e]); }
    *reinterpret_cast<f32x4*>(&s_big[w][8 * lane]) = lo;
    *reinterpret_cast<f32x4*>(&s_big[w][8 * lane + 4]) = hi;
  }
  __syncthreads();
  {
    float t = 0.f;
#pragma unroll
    for (int ww = 0; ww < 8; ++ww) t += s_big[ww][tid];
    st_sc1(&outp[(size_t)j * DM + tid], t);
    asm volatile("s_waitcnt vmcnt(0)" ::: "memory");
  }
  __syncthreads();
  if (tid == 0) __hip_atomic_fetch_add(&cnt[CNT_B + CNT_STRIDE * (j & 15)], 1u, RLX_AGENT);
  if (b >= 16) return;
  s_big[0][tid] = hv;
  if (w == 0) {
    unsigned spins = 0;
    for (;;) {
      const unsigned want = (lane < 12) ? 11u : 10u;
      const unsigned v = (lane < 16) ? __hip_atomic_load(&cnt[CNT_B + CNT_STRIDE * lane], RLX_AGENT) : 11u;
      if (__all(v >= want)) break;
      __builtin_amdgcn_s_sleep(1);
      if (++spins > SPIN_CAP) { if (lane == 0) __hip_atomic_store(&cnt[CNT_TMO], 2u, RLX_AGENT); break; }
    }
  }
  __syncthreads();
  {
    const int cc = tid & 31, pt = tid >> 5, col = 32 * b + cc;
    float vals[11];
#pragma unroll
    for (int i = 0; i < 11; ++i) { const int jj = pt + 16 * i; vals[i] = (jj < NMLP) ? ld_sc1(&outp[(size_t)jj * DM + col]) : 0.f; }
    float t = 0.f;
#pragma unroll
    for (int i = 0; i < 11; ++i) t += vals[i];
    s_big[1][tid] = t;
    __syncthreads();
    if (tid < 32) {
      float aa = 0.f;
#pragma unroll
      for (int p = 0; p < 16; ++p) aa += s_big[1][32 * p + tid];
      const float o = bfr(bfr(aa) + bf1(b2[col]));
      out[col] = bfbits(s_big[0][col] + o);
    }
  }
}

extern "C" void kernel_launch(void* const* d_in, const int* in_sizes, int n_in,
                              void* d_out, int out_size, void* d_ws, size_t ws_size,
                              hipStream_t stream) {
  const u16* x  = (const u16*)d_in[0];
  const u16* kc = (const u16*)d_in[1];
  const u16* vc = (const u16*)d_in[2];
  const u16* wq = (const u16*)d_in[3];
  const u16* wo = (const u16*)d_in[4];
  const u16* w1 = (const u16*)d_in[6];
  const u16* b1 = (const u16*)d_in[7];
  const u16* w2 = (const u16*)d_in[8];
  const u16* b2 = (const u16*)d_in[9];
  char* ws = (char*)d_ws;
  unsigned* cnt = (unsigned*)(ws);
  float* part = (float*)(ws + (64 << 10));
  float* y    = (float*)(ws + (256 << 10));
  float* outp = (float*)(ws + (1 << 20));
  k_attn<<<256, 512, 0, stream>>>(x, kc, vc, wq, part, cnt);
  k_tail<<<256, 512, 0, stream>>>(part, x, wo, w1, b1, w2, b2, y, outp, cnt, (u16*)d_out);
}
```

```cpp
#include <hip/hip_runtime.h>
#include <hip/hip_bf16.h>
#include <stdint.h>

typedef unsigned u32x4 __attribute__((ext_vector_type(4)));
typedef float f32x4 __attribute__((ext_vector_type(4)));
typedef __bf16 bf16x2v __attribute__((ext_vector_type(2)));
typedef unsigned short u16;

constexpr int DM = 512, HQ = 32, DH = 128, SK = 32768, WIN = 4096, FF = 1376;
constexpr int NSPLIT = 8, KPS = WIN / NSPLIT;
constexpr float SCALE = 0.08838834764831845f;
constexpr float EPS = 1e-5f;
constexpr int PREC = 132;

__device__ __forceinline__ float bf_lo(unsigned u) { return __uint_as_float(u << 16); }
__device__ __forceinline__ float bf_hi(unsigned u) { return __uint_as_float(u & 0xffff0000u); }
__device__ __forceinline__ float bf1(u16 u) { return __uint_as_float(((unsigned)u) << 16); }
__device__ __forceinline__ float bfr(float x) { return (float)(__bf16)x; }
__device__ __forceinline__ u16 bfbits(float x) { return __builtin_bit_cast(u16, (__bf16)x); }
__device__ __forceinline__ float dot2(unsigned a, unsigned b, float acc) {
  return __builtin_amdgcn_fdot2_f32_bf16(__builtin_bit_cast(bf16x2v, a), __builtin_bit_cast(bf16x2v, b), acc, false);
}
template <int CTRL> __device__ __forceinline__ float dpp(float x) {
  return __builtin_bit_cast(float, __builtin_amdgcn_mov_dpp(__builtin_bit_cast(int, x), CTRL, 0xf, 0xf, true));
}
__device__ __forceinline__ float row16_sum(float x) {
  x += dpp<0x128>(x); x += dpp<0x124>(x); x += dpp<0x122>(x); x += dpp<0x121>(x); return x;
}
__device__ __forceinline__ float row16_max(float x) {
  x = fmaxf(x, dpp<0x128>(x)); x = fmaxf(x, dpp<0x124>(x)); x = fmaxf(x, dpp<0x122>(x)); x = fmaxf(x, dpp<0x121>(x)); return x;
}
__device__ __forceinline__ float xr_sum(float x) { x += __shfl_xor(x, 16); x += __shfl_xor(x, 32); return x; }
__device__ __forceinline__ float xr_max(float x) { x = fmaxf(x, __shfl_xor(x, 16)); x = fmaxf(x, __shfl_xor(x, 32)); return x; }
__device__ __forceinline__ float wave_sum(float x) { return xr_sum(row16_sum(x)); }

__device__ __forceinline__ void ln_stats8(const float (&v)[8], float& m_bf, float& den) {
  float s = 0.f;
#pragma unroll
  for (int e = 0; e < 8; ++e) s += v[e];
  s = wave_sum(s);
  const float mean = s * (1.0f / 512.0f);
  float q = 0.f;
#pragma unroll
  for (int e = 0; e < 8; ++e) { const float d = v[e] - mean; q += d * d; }
  q = wave_sum(q);
  const float var = q * (1.0f / 512.0f);
  m_bf = bfr(mean);
  den = bfr(sqrtf(bfr(bfr(var) + EPS)));
}

#define KPRE 0
#define ST(i) do {} while (0)
#define STAMP_PARAM
__global__ void __launch_bounds__(512, 2) k_attn(const u16* __restrict__ x, const u16* __restrict__ kc, const u16* __restrict__ vc,
                                                 const u16* __restrict__ wq, float* __restrict__ part, unsigned* __restrict__ y, unsigned* __restrict__ outp STAMP_PARAM) {
  ST(0);
  __shared__ __attribute__((aligned(16))) float s_ln[8][64];
  __shared__ __attribute__((aligned(16))) float s_q[8][128];
  __shared__ __attribute__((aligned(16))) float s_o[8][128];
  __shared__ float s_m[8], s_l[8];
  const int b = blockIdx.x, h = b & 31, s = b >> 5;
  const int tid = threadIdx.x, lane = tid & 63, w = tid >> 6, r = lane >> 4, c = lane & 15;

  if (tid < 64) y[b * 64 + tid] = 0xFFFFFFFFu;
  if (b < FF / 8) outp[b * DM + tid] = 0xFFFFFFFFu;
  const u32x4 xv = reinterpret_cast<const u32x4*>(x)[lane];
  const u32x4* kp = reinterpret_cast<const u32x4*>(kc + ((size_t)h * SK + (SK - WIN) + s * KPS + 64 * w + r) * DH + 8 * c);
  const u32x4* vp = reinterpret_cast<const u32x4*>(vc + ((size_t)h * SK + (SK - WIN) + s * KPS + 64 * w + r) * DH + 8 * c);
  const u32x4* wqp = reinterpret_cast<const u32x4*>(wq + (size_t)(64 * w + r) * 4096 + h * 128 + 8 * c);
  u32x4 wv[16], kv[16], vv[16];
#define ISSUE_KV(j) do { if ((j) < 16) kv[(j) & 15] = __builtin_nontemporal_load(kp + (size_t)((j) & 15) * (4 * DH / 8)); \
                         else if ((j) < 32) vv[(j) & 15] = __builtin_nontemporal_load(vp + (size_t)((j) & 15) * (4 * DH / 8)); } while (0)
#pragma unroll
  for (int t = 0; t < KPRE; ++t) ISSUE_KV(t);
#pragma unroll
  for (int t = 0; t < 16; ++t) wv[t] = __builtin_nontemporal_load(wqp + (size_t)t * (4 * 4096 / 8));
  __builtin_amdgcn_sched_barrier(0);

  ST(1);
  float xf[8];
#pragma unroll
  for (int e = 0; e < 4; ++e) { xf[2 * e] = bf_lo(xv[e]); xf[2 * e + 1] = bf_hi(xv[e]); }
  float m_bf, den;
  ln_stats8(xf, m_bf, den);
  if ((lane >> 3) == w) {
#pragma unroll
    for (int e = 0; e < 8; ++e) s_ln[w][(lane & 7) * 8 + e] = bfr(bfr(xf[e] - m_bf) / den);
  }
  __builtin_amdgcn_wave_barrier();
  ST(2);
  float acc[8];
#pragma unroll
  for (int e = 0; e < 8; ++e) acc[e] = 0.f;
#pragma unroll
  for (int t = 0; t < 16; ++t) {
    const float lv = s_ln[w][4 * t + r];
#pragma unroll
    for (int e = 0; e < 4; ++e) { acc[2 * e] = fmaf(lv, bf_lo(wv[t][e]), acc[2 * e]); acc[2 * e + 1] = fmaf(lv, bf_hi(wv[t][e]), acc[2 * e + 1]); }
    ISSUE_KV(KPRE + t);
    __builtin_amdgcn_sched_barrier(0);
  }
#pragma unroll
  for (int e = 0; e < 8; ++e) acc[e] = xr_sum(acc[e]);
  if (r == 0) {
    *reinterpret_cast<f32x4*>(&s_q[w][8 * c]) = f32x4{acc[0], acc[1], acc[2], acc[3]};
    *reinterpret_cast<f32x4*>(&s_q[w][8 * c + 4]) = f32x4{acc[4], acc[5], acc[6], acc[7]};
  }
  ST(3);
  __syncthreads();
  float qf[8];
#pragma unroll
  for (int e = 0; e < 8; ++e) qf[e] = 0.f;
#pragma unroll
  for (int ww = 0; ww < 8; ++ww) {
    const f32x4 a = *reinterpret_cast<const f32x4*>(&s_q[ww][8 * c]);
    const f32x4 bq = *reinterpret_cast<const f32x4*>(&s_q[ww][8 * c + 4]);
#pragma unroll
    for (int e = 0; e < 4; ++e) { qf[e] += a[e]; qf[4 + e] += bq[e]; }
  }
  unsigned qpk[4];
#pragma unroll
  for (int e = 0; e < 4; ++e) qpk[e] = (unsigned)bfbits(qf[2 * e]) | ((unsigned)bfbits(qf[2 * e + 1]) << 16);
  ST(4);
  float sc[16];
#pragma unroll
  for (int t = 0; t < 16; ++t) {
    float d = 0.f;
#pragma unroll
    for (int e = 0; e < 4; ++e) d = dot2(kv[t][e], qpk[e], d);
    d = row16_sum(d);
    sc[t] = bfr(bfr(d) * SCALE);
    ISSUE_KV(KPRE + 16 + t);
    __builtin_amdgcn_sched_barrier(0);
  }
#undef ISSUE_KV
  float mx = sc[0];
#pragma unroll
  for (int t = 1; t < 16; ++t) mx = fmaxf(mx, sc[t]);
  mx = xr_max(mx);
  ST(5);
  float ls = 0.f;
  float oa[8];
#pragma unroll
  for (int e = 0; e < 8; ++e) oa[e] = 0.f;
#pragma unroll
  for (int t = 0; t < 16; ++t) {
    const float p = __builtin_amdgcn_exp2f((sc[t] - mx) * 1.4426950408889634f);
    ls += p;
#pragma unroll
    for (int e = 0; e < 4; ++e) { oa[2 * e] = fmaf(p, bf_lo(vv[t][e]), oa[2 * e]); oa[2 * e + 1] = fmaf(p, bf_hi(vv[t][e]), oa[2 * e + 1]); }
  }
  ST(6);
  ls = xr_sum(ls);
#pragma unroll
  for (int e = 0; e < 8; ++e) oa[e] = xr_sum(oa[e]);
  if (r == 0) {
    *reinterpret_cast<f32x4*>(&s_o[w][8 * c]) = f32x4{oa[0], oa[1], oa[2], oa[3]};
    *reinterpret_cast<f32x4*>(&s_o[w][8 * c + 4]) = f32x4{oa[4], oa[5], oa[6], oa[7]};
    if (c == 0) { s_m[w] = mx; s_l[w] = ls; }
  }
  __syncthreads();
  if (tid < 128) {
    float M = s_m[0];
#pragma unroll
    for (int ww = 1; ww < 8; ++ww) M = fmaxf(M, s_m[ww]);
    float L = 0.f, o = 0.f;
#pragma unroll
    for (int ww = 0; ww < 8; ++ww) {
      const float e = __builtin_amdgcn_exp2f((s_m[ww] - M) * 1.4426950408889634f);
      L = fmaf(e, s_l[ww], L); o = fmaf(e, s_o[ww][tid], o);
    }
    float* rec = part + (size_t)(h * NSPLIT + s) * PREC;
    rec[4 + tid] = o;
    if (tid == 0) { rec[0] = M; rec[1] = L; }
  }
}

#define RLX_AGENT __ATOMIC_RELAXED, __HIP_MEMORY_SCOPE_AGENT
constexpr int NMLP = FF / 8;
constexpr unsigned SPIN_CAP = 1u << 20;
constexpr unsigned SENT = 0xFFFFFFFFu;
__device__ __forceinline__ unsigned ld_sc1u(const unsigned* p) { return __hip_atomic_load(p, RLX_AGENT); }
__device__ __forceinline__ void st_tag(unsigned* p, float v) {
  unsigned u = __float_as_uint(v); u = (u == SENT) ? 0x7FC00000u : u; __hip_atomic_store(p, u, RLX_AGENT);
}

__global__ void __launch_bounds__(512) k_tail(const float* __restrict__ part, const u16* __restrict__ x, const u16* __restrict__ wo,
                                              const u16* __restrict__ w1, const u16* __restrict__ b1, const u16* __restrict__ w2, const u16* __restrict__ b2,
                                              unsigned* y, unsigned* outp, u16* __restrict__ out STAMP_PARAM) {
#define STFLUSH() do {} while (0)
  ST(0);
  __shared__ __attribute__((aligned(16))) float s_big[8][512];
  __shared__ __attribute__((aligned(16))) float s_h[512];
  __shared__ float s_o[128];
  __shared__ float s_red[8][64];
  const int b = blockIdx.x, h = b & 31, s = b >> 5;
  const int tid = threadIdx.x, lane = tid & 63, w = tid >> 6;
  const bool mlp = b < NMLP;
  float pm[NSPLIT], pl[NSPLIT], po[NSPLIT];
  {
    const float* p = part + (size_t)h * NSPLIT * PREC;
    const int d = tid & 127;
#pragma unroll
    for (int ss = 0; ss < NSPLIT; ++ss) { pm[ss] = p[ss * PREC]; pl[ss] = p[ss * PREC + 1]; po[ss] = p[ss * PREC + 4 + d]; }
  }
  u32x4 wv[2];
#pragma unroll
  for (int i = 0; i < 2; ++i)
    wv[i] = __builtin_nontemporal_load(reinterpret_cast<const u32x4*>(wo + (size_t)(h * 128 + 16 * w + 8 * i + (lane >> 3)) * DM + 64 * s + 8 * (lane & 7)));
  const int j = mlp ? b : 0;
  const u32x4 w1v = __builtin_nontemporal_load(reinterpret_cast<const u32x4*>(w1 + (size_t)tid * FF + 8 * j));
  const u32x4 w2v = __builtin_nontemporal_load(reinterpret_cast<const u32x4*>(w2 + (size_t)(8 * j) * DM + 8 * tid));
  const float xv = bf1(x[tid]);
  const float b1v = bf1(b1[8 * j + w]);
  __builtin_amdgcn_sched_barrier(0);
  {
    float M = pm[0];
#pragma unroll
    for (int ss = 1; ss < NSPLIT; ++ss) M = fmaxf(M, pm[ss]);
    float L = 0.f, o = 0.f;
#pragma unroll
    for (int ss = 0; ss < NSPLIT; ++ss) {
      const float e = __builtin_amdgcn_exp2f((pm[ss] - M) * 1.4426950408889634f);
      L = fmaf(e, pl[ss], L); o = fmaf(e, po[ss], o);
    }
    if (tid < 128) s_o[tid] = bfr(o / L);
  }
  __syncthreads();
  ST(1);
  {
    float acc[8];
#pragma unroll
    for (int e = 0; e < 8; ++e) acc[e] = 0.f;
#pragma unroll
    for (int i = 0; i < 2; ++i) {
      const float ov = s_o[16 * w + 8 * i + (lane >> 3)];
#pragma unroll
      for (int e = 0; e < 4; ++e) { acc[2 * e] = fmaf(ov, bf_lo(wv[i][e]), acc[2 * e]); acc[2 * e + 1] = fmaf(ov, bf_hi(wv[i][e]), acc[2 * e + 1]); }
    }
#pragma unroll
    for (int e = 0; e < 8; ++e) { acc[e] += dpp<0x128>(acc[e]); acc[e] += __shfl_xor(acc[e], 16); acc[e] += __shfl_xor(acc[e], 32); }
    if (lane < 8) {
#pragma unroll
      for (int e = 0; e < 8; ++e) s_red[w][8 * lane + e] = acc[e];
    }
  }
  __syncthreads();
  if (tid < 64) {
    float t = 0.f;
#pragma unroll
    for (int ww = 0; ww < 8; ++ww) t += s_red[ww][tid];
    st_tag(&y[h * DM + 64 * s + tid], t);
  }
  ST(2);
  if (!mlp) { STFLUSH(); return; }
  float a = 0.f;
  {
    unsigned yv[HQ];
    unsigned spins = 0;
    for (;;) {
      bool ok = true;
#pragma unroll
      for (int hh = 0; hh < HQ; ++hh) { yv[hh] = ld_sc1u(&y[hh * DM + tid]); ok &= (yv[hh] != SENT); }
      if (__all(ok)) break;
      if (++spins > SPIN_CAP) break;
      __builtin_amdgcn_s_sleep(1);
    }
    ST(3);
#pragma unroll
    for (int hh = 0; hh < HQ; ++hh) a += __uint_as_float(yv[hh]);
  }
  const float hv = bfr(xv + bfr(a));
  s_h[tid] = hv;
  __syncthreads();
  ST(4);
  float ln;
  {
    float v8[8];
    const f32x4 h0 = *reinterpret_cast<const f32x4*>(&s_h[8 * lane]), h1 = *reinterpret_cast<const f32x4*>(&s_h[8 * lane + 4]);
#pragma unroll
    for (int e = 0; e < 4; ++e) { v8[e] = h0[e]; v8[4 + e] = h1[e]; }
    float m_bf, den;
    ln_stats8(v8, m_bf, den);
    ln = bfr(bfr(hv - m_bf) / den);
  }
  {
    float acc[8];
#pragma unroll
    for (int e = 0; e < 4; ++e) { acc[2 * e] = ln * bf_lo(w1v[e]); acc[2 * e + 1] = ln * bf_hi(w1v[e]); }
#pragma unroll
    for (int e = 0; e < 8; ++e) acc[e] = wave_sum(acc[e]);
    if (lane == 0) {
#pragma unroll
      for (int e = 0; e < 8; ++e) s_red[w][e] = acc[e];
    }
  }
  __syncthreads();
  {
    float u = 0.f;
#pragma unroll
    for (int ww = 0; ww < 8; ++ww) u += s_red[ww][w];
    u = bfr(bfr(u) + b1v);
    const float rr = u > 0.f ? u : 0.f;
    const float uu = bfr(rr * rr);
    f32x4 lo, hi;
#pragma unroll
    for (int e = 0; e < 2; ++e) { lo[2 * e] = uu * bf_lo(w2v[e]); lo[2 * e + 1] = uu * bf_hi(w2v[e]); hi[2 * e] = uu * bf_lo(w2v[2 + e]); hi[2 * e + 1] = uu * bf_hi(w2v[2 + e]); }
    *reinterpret_cast<f32x4*>(&s_big[w][8 * lane]) = lo;
    *reinterpret_cast<f32x4*>(&s_big[w][8 * lane + 4]) = hi;
  }
  __syncthreads();
  {
    float t = 0.f;
#pragma unroll
    for (int ww = 0; ww < 8; ++ww) t += s_big[ww][tid];
    st_tag(&outp[(size_t)j * DM + tid], t);
  }
  ST(5);
  if (b >= 16) { STFLUSH(); return; }
  {
    const int cc = tid & 31, pt = tid >> 5, col = 32 * b + cc;
    unsigned vals[11];
    unsigned spins = 0;
    for (;;) {
      bool ok = true;
#pragma unroll
      for (int i = 0; i < 11; ++i) { const int jj = pt + 16 * i; vals[i] = (jj < NMLP) ? ld_sc1u(&outp[(size_t)jj * DM + col]) : 0u; ok &= (vals[i] != SENT); }
      if (__all(ok)) break;
      if (++spins > SPIN_CAP) break;
      __builtin_amdgcn_s_sleep(1);
    }
    ST(6);
    float t = 0.f;
#pragma unroll
    for (int i = 0; i < 11; ++i) t += __uint_as_float(vals[i]);
    __syncthreads();
    s_big[1][tid] = t;
    __syncthreads();
    if (tid < 32) {
      float aa = 0.f;
#pragma unroll
      for (int p = 0; p < 16; ++p) aa += s_big[1][32 * p + tid];
      const float o = bfr(bfr(aa) + bf1(b2[col]));
      out[col] = bfbits(s_h[col] + o);
    }
  }
  ST(7); STFLUSH();
}

extern "C" void kernel_launch(void* const* d_in, const int* in_sizes, int n_in,
                              void* d_out, int out_size, void* d_ws, size_t ws_size,
                              hipStream_t stream) {
  const u16* x  = (const u16*)d_in[0];
  const u16* kc = (const u16*)d_in[1];
  const u16* vc = (const u16*)d_in[2];
  const u16* wq = (const u16*)d_in[3];
  const u16* wo = (const u16*)d_in[4];
  const u16* w1 = (const u16*)d_in[6];
  const u16* b1 = (const u16*)d_in[7];
  const u16* w2 = (const u16*)d_in[8];
  const u16* b2 = (const u16*)d_in[9];
  char* ws = (char*)d_ws;
  float* part = (float*)(ws + (64 << 10));
  unsigned* y    = (unsigned*)(ws + (256 << 10));
  unsigned* outp = (unsigned*)(ws + (1 << 20));
  k_attn<<<256, 512, 0, stream>>>(x, kc, vc, wq, part, y, outp);
  k_tail<<<256, 512, 0, stream>>>(part, x, wo, w1, b1, w2, b2, y, outp, (u16*)d_out);
}
```

```cpp
#include <hip/hip_runtime.h>
#include <hip/hip_bf16.h>
#include <stdint.h>

typedef unsigned u32x4 __attribute__((ext_vector_type(4)));
typedef float f32x4 __attribute__((ext_vector_type(4)));
typedef __bf16 bf16x2v __attribute__((ext_vector_type(2)));
typedef unsigned short u16;

constexpr int DM = 512, HQ = 32, DH = 128, SK = 32768, WIN = 4096, FF = 1376;
constexpr int NSPLIT = 8, KPS = WIN / NSPLIT;
constexpr float SCALE = 0.08838834764831845f;
constexpr float EPS = 1e-5f;
constexpr int PREC = 132;

__device__ __forceinline__ float bf_lo(unsigned u) { return __uint_as_float(u << 16); }
__device__ __forceinline__ float bf_hi(unsigned u) { return __uint_as_float(u & 0xffff0000u); }
__device__ __forceinline__ float bf1(u16 u) { return __uint_as_float(((unsigned)u) << 16); }
__device__ __forceinline__ float bfr(float x) { return (float)(__bf16)x; }
__device__ __forceinline__ u16 bfbits(float x) { return __builtin_bit_cast(u16, (__bf16)x); }
__device__ __forceinline__ float dot2(unsigned a, unsigned b, float acc) {
  return __builtin_amdgcn_fdot2_f32_bf16(__builtin_bit_cast(bf16x2v, a), __builtin_bit_cast(bf16x2v, b), acc, false);
}
template <int CTRL> __device__ __forceinline__ float dpp(float x) {
  return __builtin_bit_cast(float, __builtin_amdgcn_mov_dpp(__builtin_bit_cast(int, x), CTRL, 0xf, 0xf, true));
}
__device__ __forceinline__ float row16_sum(float x) {
  x += dpp<0x128>(x); x += dpp<0x124>(x); x += dpp<0x122>(x); x += dpp<0x121>(x); return x;
}
__device__ __forceinline__ float row16_max(float x) {
  x = fmaxf(x, dpp<0x128>(x)); x = fmaxf(x, dpp<0x124>(x)); x = fmaxf(x, dpp<0x122>(x)); x = fmaxf(x, dpp<0x121>(x)); return x;
}
__device__ __forceinline__ float xr_sum(float x) { x += __shfl_xor(x, 16); x += __shfl_xor(x, 32); return x; }
__device__ __forceinline__ float xr_max(float x) { x = fmaxf(x, __shfl_xor(x, 16)); x = fmaxf(x, __shfl_xor(x, 32)); return x; }
__device__ __forceinline__ float wave_sum(float x) { return xr_sum(row16_sum(x)); }

__device__ __forceinline__ void ln_stats8(const float (&v)[8], float& m_bf, float& den) {
  float s = 0.f;
#pragma unroll
  for (int e = 0; e < 8; ++e) s += v[e];
  s = wave_sum(s);
  const float mean = s * (1.0f / 512.0f);
  float q = 0.f;
#pragma unroll
  for (int e = 0; e < 8; ++e) { const float d = v[e] - mean; q += d * d; }
  q = wave_sum(q);
  const float var = q * (1.0f / 512.0f);
  m_bf = bfr(mean);
  den = bfr(sqrtf(bfr(bfr(var) + EPS)));
}

#define KPRE 0
#define QISS 0
#define ST(i) do {} while (0)
#define STAMP_PARAM
__global__ void __launch_bounds__(512, 2) k_attn(const u16* __restrict__ x, const u16* __restrict__ kc, const u16* __restrict__ vc,
                                                 const u16* __restrict__ wq, float* __restrict__ part, unsigned* __restrict__ y, unsigned* __restrict__ outp STAMP_PARAM) {
  ST(0);
  __shared__ __attribute__((aligned(16))) float s_ln[8][64];
  __shared__ __attribute__((aligned(16))) float s_q[8][128];
  __shared__ __attribute__((aligned(16))) float s_o[8][128];
  __shared__ float s_m[8], s_l[8];
  const int b = blockIdx.x, h = b & 31, s = b >> 5;
  const int tid = threadIdx.x, lane = tid & 63, w = tid >> 6, r = lane >> 4, c = lane & 15;

  if (tid < 64) y[b * 64 + tid] = 0xFFFFFFFFu;
  if (b < FF / 8) outp[b * DM + tid] = 0xFFFFFFFFu;
  const u32x4 xv = reinterpret_cast<const u32x4*>(x)[lane];
  const u32x4* kp = reinterpret_cast<const u32x4*>(kc + ((size_t)h * SK + (SK - WIN) + s * KPS + 64 * w + r) * DH + 8 * c);
  const u32x4* vp = reinterpret_cast<const u32x4*>(vc + ((size_t)h * SK + (SK - WIN) + s * KPS + 64 * w + r) * DH + 8 * c);
  const u32x4* wqp = reinterpret_cast<const u32x4*>(wq + (size_t)(64 * w + r) * 4096 + h * 128 + 8 * c);
  u32x4 wv[16], kv[16], vv[16];
#define ISSUE_KV(j) do { if ((j) < 16) kv[(j) & 15] = __builtin_nontemporal_load(kp + (size_t)((j) & 15) * (4 * DH / 8)); \
                         else if ((j) < 32) vv[(j) & 15] = __builtin_nontemporal_load(vp + (size_t)((j) & 15) * (4 * DH / 8)); } while (0)
#pragma unroll
  for (int t = 0; t < KPRE; ++t) ISSUE_KV(t);
#pragma unroll
  for (int t = 0; t < 16; ++t) wv[t] = __builtin_nontemporal_load(wqp + (size_t)t * (4 * 4096 / 8));
  __builtin_amdgcn_sched_barrier(0);

  ST(1);
  float xf[8];
#pragma unroll
  for (int e = 0; e < 4; ++e) { xf[2 * e] = bf_lo(xv[e]); xf[2 * e + 1] = bf_hi(xv[e]); }
  float m_bf, den;
  ln_stats8(xf, m_bf, den);
  if ((lane >> 3) == w) {
#pragma unroll
    for (int e = 0; e < 8; ++e) s_ln[w][(lane & 7) * 8 + e] = bfr(bfr(xf[e] - m_bf) / den);
  }
  __builtin_amdgcn_wave_barrier();
  ST(2);
  float acc[8];
#pragma unroll
  for (int e = 0; e < 8; ++e) acc[e] = 0.f;
#pragma unroll
  for (int t = 0; t < 16; ++t) {
    const float lv = s_ln[w][4 * t + r];
#pragma unroll
    for (int e = 0; e < 4; ++e) { acc[2 * e] = fmaf(lv, bf_lo(wv[t][e]), acc[2 * e]); acc[2 * e + 1] = fmaf(lv, bf_hi(wv[t][e]), acc[2 * e + 1]); }
    __builtin_amdgcn_sched_barrier(0);
  }
#pragma unroll
  for (int t = 0; t < 16; ++t) ISSUE_KV(KPRE + t);
#pragma unroll
  for (int e = 0; e < 8; ++e) acc[e] = xr_sum(acc[e]);
  if (r == 0) {
    *reinterpret_cast<f32x4*>(&s_q[w][8 * c]) = f32x4{acc[0], acc[1], acc[2], acc[3]};
    *reinterpret_cast<f32x4*>(&s_q[w][8 * c + 4]) = f32x4{acc[4], acc[5], acc[6], acc[7]};
  }
  ST(3);
  __syncthreads();
  float qf[8];
#pragma unroll
  for (int e = 0; e < 8; ++e) qf[e] = 0.f;
#pragma unroll
  for (int ww = 0; ww < 8; ++ww) {
    const f32x4 a = *reinterpret_cast<const f32x4*>(&s_q[ww][8 * c]);
    const f32x4 bq = *reinterpret_cast<const f32x4*>(&s_q[ww][8 * c + 4]);
#pragma unroll
    for (int e = 0; e < 4; ++e) { qf[e] += a[e]; qf[4 + e] += bq[e]; }
  }
  unsigned qpk[4];
#pragma unroll
  for (int e = 0; e < 4; ++e) qpk[e] = (unsigned)bfbits(qf[2 * e]) | ((unsigned)bfbits(qf[2 * e + 1]) << 16);
  ST(4);
  float sc[16];
#pragma unroll
  for (int t = 0; t < 16; ++t) {
    float d = 0.f;
#pragma unroll
    for (int e = 0; e < 4; ++e) d = dot2(kv[t][e], qpk[e], d);
    d = row16_sum(d);
    sc[t] = bfr(bfr(d) * SCALE);
    ISSUE_KV(KPRE + 16 + t);
    __builtin_amdgcn_sched_barrier(0);
  }
#undef ISSUE_KV
  float mx = sc[0];
#pragma unroll
  for (int t = 1; t < 16; ++t) mx = fmaxf(mx, sc[t]);
  mx = xr_max(mx);
  ST(5);
  float ls = 0.f;
  float oa[8];
#pragma unroll
  for (int e = 0; e < 8; ++e) oa[e] = 0.f;
#pragma unroll
  for (int t = 0; t < 16; ++t) {
    const float p = __builtin_amdgcn_exp2f((sc[t] - mx) * 1.4426950408889634f);
    ls += p;
#pragma unroll
    for (int e = 0; e < 4; ++e) { oa[2 * e] = fmaf(p, bf_lo(vv[t][e]), oa[2 * e]); oa[2 * e + 1] = fmaf(p, bf_hi(vv[t][e]), oa[2 * e + 1]); }
  }
  ST(6);
  ls = xr_sum(ls);
#pragma unroll
  for (int e = 0; e < 8; ++e) oa[e] = xr_sum(oa[e]);
  if (r == 0) {
    *reinterpret_cast<f32x4*>(&s_o[w][8 * c]) = f32x4{oa[0], oa[1], oa[2], oa[3]};
    *reinterpret_cast<f32x4*>(&s_o[w][8 * c + 4]) = f32x4{oa[4], oa[5], oa[6], oa[7]};
    if (c == 0) { s_m[w] = mx; s_l[w] = ls; }
  }
  __syncthreads();
  if (tid < 128) {
    float M = s_m[0];
#pragma unroll
    for (int ww = 1; ww < 8; ++ww) M = fmaxf(M, s_m[ww]);
    float L = 0.f, o = 0.f;
#pragma unroll
    for (int ww = 0; ww < 8; ++ww) {
      const float e = __builtin_amdgcn_exp2f((s_m[ww] - M) * 1.4426950408889634f);
      L = fmaf(e, s_l[ww], L); o = fmaf(e, s_o[ww][tid], o);
    }
    float* rec = part + (size_t)(h * NSPLIT + s) * PREC;
    rec[4 + tid] = o;
    if (tid == 0) { rec[0] = M; rec[1] = L; }
  }
}

#define RLX_AGENT __ATOMIC_RELAXED, __HIP_MEMORY_SCOPE_AGENT
constexpr int NMLP = FF / 8;
constexpr unsigned SPIN_CAP = 1u << 20;
constexpr unsigned SENT = 0xFFFFFFFFu;
__device__ __forceinline__ unsigned ld_sc1u(const unsigned* p) { return __hip_atomic_load(p, RLX_AGENT); }
__device__ __forceinline__ void st_tag(unsigned* p, float v) {
  unsigned u = __float_as_uint(v); u = (u == SENT) ? 0x7FC00000u : u; __hip_atomic_store(p, u, RLX_AGENT);
}

__global__ void __launch_bounds__(512) k_tail(const float* __restrict__ part, const u16* __restrict__ x, const u16* __restrict__ wo,
                                              const u16* __restrict__ w1, const u16* __restrict__ b1, const u16* __restrict__ w2, const u16* __restrict__ b2,
                                              unsigned* y, unsigned* outp, u16* __restrict__ out STAMP_PARAM) {
#define STFLUSH() do {} while (0)
  ST(0);
  __shared__ __attribute__((aligned(16))) float s_big[8][512];
  __shared__ __attribute__((aligned(16))) float s_h[512];
  __shared__ float s_red[8][64];
  const int b = blockIdx.x, h = b & 31, s = b >> 5;
  const int tid = threadIdx.x, lane = tid & 63, w = tid >> 6;
  const bool mlp = b < NMLP;
  float pm[NSPLIT], pl[NSPLIT], po0[NSPLIT], po1[NSPLIT];
  {
    const float* p = part + (size_t)h * NSPLIT * PREC;
    const int d = 16 * w + (lane >> 3);
#pragma unroll
    for (int ss = 0; ss < NSPLIT; ++ss) { pm[ss] = p[ss * PREC]; pl[ss] = p[ss * PREC + 1]; po0[ss] = p[ss * PREC + 4 + d]; po1[ss] = p[ss * PREC + 12 + d]; }
  }
  u32x4 wv[2];
#pragma unroll
  for (int i = 0; i < 2; ++i)
    wv[i] = __builtin_nontemporal_load(reinterpret_cast<const u32x4*>(wo + (size_t)(h * 128 + 16 * w + 8 * i + (lane >> 3)) * DM + 64 * s + 8 * (lane & 7)));
  const int j = mlp ? b : 0;
  const u32x4 w1v = __builtin_nontemporal_load(reinterpret_cast<const u32x4*>(w1 + (size_t)tid * FF + 8 * j));
  const u32x4 w2v = __builtin_nontemporal_load(reinterpret_cast<const u32x4*>(w2 + (size_t)(8 * j) * DM + 8 * tid));
  const float xv = bf1(x[tid]);
  const float b1v = bf1(b1[8 * j + w]);
  __builtin_amdgcn_sched_barrier(0);
  float ovv[2];
  {
    float M = pm[0];
#pragma unroll
    for (int ss = 1; ss < NSPLIT; ++ss) M = fmaxf(M, pm[ss]);
    float L = 0.f, o0 = 0.f, o1 = 0.f;
#pragma unroll
    for (int ss = 0; ss < NSPLIT; ++ss) {
      const float e = __builtin_amdgcn_exp2f((pm[ss] - M) * 1.4426950408889634f);
      L = fmaf(e, pl[ss], L); o0 = fmaf(e, po0[ss], o0); o1 = fmaf(e, po1[ss], o1);
    }
    ovv[0] = bfr(o0 / L); ovv[1] = bfr(o1 / L);
  }
  ST(1);
  {
    float acc[8];
#pragma unroll
    for (int e = 0; e < 8; ++e) acc[e] = 0.f;
#pragma unroll
    for (int i = 0; i < 2; ++i) {
      const float ov = ovv[i];
#pragma unroll
      for (int e = 0; e < 4; ++e) { acc[2 * e] = fmaf(ov, bf_lo(wv[i][e]), acc[2 * e]); acc[2 * e + 1] = fmaf(ov, bf_hi(wv[i][e]), acc[2 * e + 1]); }
    }
#pragma unroll
    for (int e = 0; e < 8; ++e) { acc[e] += dpp<0x128>(acc[e]); acc[e] += __shfl_xor(acc[e], 16); acc[e] += __shfl_xor(acc[e], 32); }
    if (lane < 8) {
#pragma unroll
      for (int e = 0; e < 8; ++e) s_red[w][8 * lane + e] = acc[e];
    }
  }
  __syncthreads();
  if (tid < 64) {
    float t = 0.f;
#pragma unroll
    for (int ww = 0; ww < 8; ++ww) t += s_red[ww][tid];
    st_tag(&y[h * DM + 64 * s + tid], t);
  }
  ST(2);
  if (!mlp) { STFLUSH(); return; }
  float a = 0.f;
  {
    unsigned yv[HQ];
    unsigned spins = 0;
    for (;;) {
      bool ok = true;
#pragma unroll
      for (int hh = 0; hh < HQ; ++hh) { yv[hh] = ld_sc1u(&y[hh * DM + tid]); ok &= (yv[hh] != SENT); }
      if (__all(ok)) break;
      if (++spins > SPIN_CAP) break;
      __builtin_amdgcn_s_sleep(1);
    }
    ST(3);
#pragma unroll
    for (int hh = 0; hh < HQ; ++hh) a += __uint_as_float(yv[hh]);
  }
  const float hv = bfr(xv + bfr(a));
  s_h[tid] = hv;
  __syncthreads();
  ST(4);
  float ln;
  {
    float v8[8];
    const f32x4 h0 = *reinterpret_cast<const f32x4*>(&s_h[8 * lane]), h1 = *reinterpret_cast<const f32x4*>(&s_h[8 * lane + 4]);
#pragma unroll
    for (int e = 0; e < 4; ++e) { v8[e] = h0[e]; v8[4 + e] = h1[e]; }
    float m_bf, den;
    ln_stats8(v8, m_bf, den);
    ln = bfr(bfr(hv - m_bf) / den);
  }
  {
    float acc[8];
#pragma unroll
    for (int e = 0; e < 4; ++e) { acc[2 * e] = ln * bf_lo(w1v[e]); acc[2 * e + 1] = ln * bf_hi(w1v[e]); }
#pragma unroll
    for (int e = 0; e < 8; ++e) acc[e] = wave_sum(acc[e]);
    if (lane == 0) {
#pragma unroll
      for (int e = 0; e < 8; ++e) s_red[w][e] = acc[e];
    }
  }
  __syncthreads();
  {
    float u = 0.f;
#pragma unroll
    for (int ww = 0; ww < 8; ++ww) u += s_red[ww][w];
    u = bfr(bfr(u) + b1v);
    const float rr = u > 0.f ? u : 0.f;
    const float uu = bfr(rr * rr);
    f32x4 lo, hi;
#pragma unroll
    for (int e = 0; e < 2; ++e) { lo[2 * e] = uu * bf_lo(w2v[e]); lo[2 * e + 1] = uu * bf_hi(w2v[e]); hi[2 * e] = uu * bf_lo(w2v[2 + e]); hi[2 * e + 1] = uu * bf_hi(w2v[2 + e]); }
    *reinterpret_cast<f32x4*>(&s_big[w][8 * lane]) = lo;
    *reinterpret_cast<f32x4*>(&s_big[w][8 * lane + 4]) = hi;
  }
  __syncthreads();
  {
    float t = 0.f;
#pragma unroll
    for (int ww = 0; ww < 8; ++ww) t += s_big[ww][tid];
    st_tag(&outp[(size_t)j * DM + tid], t);
  }
  ST(5);
  if (b >= 16) { STFLUSH(); return; }
  {
    const int cc = tid & 31, pt = tid >> 5, col = 32 * b + cc;
    unsigned vals[11];
    unsigned spins = 0;
    for (;;) {
      bool ok = true;
#pragma unroll
      for (int i = 0; i < 11; ++i) { const int jj = pt + 16 * i; vals[i] = (jj < NMLP) ? ld_sc1u(&outp[(size_t)jj * DM + col]) : 0u; ok &= (vals[i] != SENT); }
      if (__all(ok)) break;
      if (++spins > SPIN_CAP) break;
      __builtin_amdgcn_s_sleep(1);
    }
    ST(6);
    float t = 0.f;
#pragma unroll
    for (int i = 0; i < 11; ++i) t += __uint_as_float(vals[i]);
    __syncthreads();
    s_big[1][tid] = t;
    __syncthreads();
    if (tid < 32) {
      float aa = 0.f;
#pragma unroll
      for (int p = 0; p < 16; ++p) aa += s_big[1][32 * p + tid];
      const float o = bfr(bfr(aa) + bf1(b2[col]));
      out[col] = bfbits(s_h[col] + o);
    }
  }
  ST(7); STFLUSH();
}

extern "C" void kernel_launch(void* const* d_in, const int* in_sizes, int n_in,
                              void* d_out, int out_size, void* d_ws, size_t ws_size,
                              hipStream_t stream) {
  const u16* x  = (const u16*)d_in[0];
  const u16* kc = (const u16*)d_in[1];
  const u16* vc = (const u16*)d_in[2];
  const u16* wq = (const u16*)d_in[3];
  const u16* wo = (const u16*)d_in[4];
  const u16* w1 = (const u16*)d_in[6];
  const u16* b1 = (const u16*)d_in[7];
  const u16* w2 = (const u16*)d_in[8];
  const u16* b2 = (const u16*)d_in[9];
  char* ws = (char*)d_ws;
  float* part = (float*)(ws + (64 << 10));
  unsigned* y    = (unsigned*)(ws + (256 << 10));
  unsigned* outp = (unsigned*)(ws + (1 << 20));
  k_attn<<<256, 512, 0, stream>>>(x, kc, vc, wq, part, y, outp);
  k_tail<<<256, 512, 0, stream>>>(part, x, wo, w1, b1, w2, b2, y, outp, (u16*)d_out);
}
```
